```python
import torch

def _softmax(x, axis=-1):
    return torch.softmax(x, dim=axis)
def _gelu(x):
    return torch.nn.functional.gelu(x, approximate="tanh")
def _silu(x):
    return torch.nn.functional.silu(x)
def _sigmoid(x):
    return torch.sigmoid(x)
def _layernorm(x, axis=-1, eps=1e-5):
    m = x.mean(dim=axis, keepdim=True)
    v = x.var(dim=axis, keepdim=True, unbiased=False)
    return (x - m) / torch.sqrt(v + eps)
def _scatter_add(tgt, idx, src, axis=0):
    return tgt.clone().index_add_(axis, idx, src)
def _segment_sum(src, seg, n):
    out = torch.zeros((n,) + src.shape[1:], dtype=src.dtype)
    return out.index_add_(0, seg, src)
def _pad(x, pw):
    flat = tuple(v for pair in reversed(pw) for v in pair)
    return torch.nn.functional.pad(x, flat)

kernel_name = 'concat_heads__mha_block_cached__gqa_block_cached__near_ridge'

def setup_inputs():

    _w = (lambda a: (lambda t: t.to(torch.bfloat16) if t.is_floating_point() else t)(torch.as_tensor(a)))
    def _A_setup():
        B, Sq, D = 8, 96, 448
        H, Sk, Dh = 16, 512, 256
        return {
            "x": _w((lambda *s: torch.randn(*s, dtype=torch.bfloat16) * 0.8)(B, Sq, D)),
            "k_cache": _w((lambda *s: torch.randn(*s, dtype=torch.bfloat16) * 0.8)(B, H, Sk, Dh)),
            "v_cache": _w((lambda *s: torch.randn(*s, dtype=torch.bfloat16) * 0.05)(B, H, Sk, Dh)),
            "wq": _w((lambda *s: torch.randn(*s, dtype=torch.bfloat16) * 0.8)(D, H * Dh) * (D ** -0.5)),
            "wo": _w((lambda *s: torch.randn(*s, dtype=torch.bfloat16) * 0.05)(H * Dh, D) * ((H * Dh) ** -0.5)),
        }
    _A_in = _A_setup()

    def _B_setup():
        B, Sq, D = 8, 96, 448
        Hq, Hkv, Sk, Dh = 16, 16, 512, 256
        assert Hq % Hkv == 0, (Hq, Hkv)
        return {
            "x": _w((lambda *s: torch.randn(*s, dtype=torch.bfloat16) * 0.8)(B, Sq, D)),
            "k_cache": _w((lambda *s: torch.randn(*s, dtype=torch.bfloat16) * 0.8)(B, Hkv, Sk, Dh)),
            "v_cache": _w((lambda *s: torch.randn(*s, dtype=torch.bfloat16) * 0.05)(B, Hkv, Sk, Dh)),
            "wq": _w((lambda *s: torch.randn(*s, dtype=torch.bfloat16) * 0.8)(D, Hq * Dh) * (D ** -0.5)),
            "wo": _w((lambda *s: torch.randn(*s, dtype=torch.bfloat16) * 0.05)(Hq * Dh, D) * ((Hq * Dh) ** -0.5)),
        }
    _B_in = _B_setup()

    return {
        "A_x": _A_in["x"],
        "A_k_cache": _A_in["k_cache"],
        "A_v_cache": _A_in["v_cache"],
        "A_wq": _A_in["wq"],
        "A_wo": _A_in["wo"],
        "B_x": _B_in["x"],
        "B_k_cache": _B_in["k_cache"],
        "B_v_cache": _B_in["v_cache"],
        "B_wq": _B_in["wq"],
        "B_wo": _B_in["wo"],
    }

def reference(A_x, A_k_cache, A_v_cache, A_wq, A_wo, B_x, B_k_cache, B_v_cache, B_wq, B_wo):
    def _A_ref(x, k_cache, v_cache, wq, wo):
        B, Sq, D = x.shape
        H, Dh = 16, 256
        q = (lambda x, *d: x.permute(*d))((x @ wq).reshape(B, Sq, H, Dh), 0, 2, 1, 3)
        scores = torch.einsum("bhqd,bhkd->bhqk", q, k_cache) * (Dh ** -0.5)
        p = _softmax(scores, axis=-1)
        o = torch.einsum("bhqk,bhkd->bhqd", p, v_cache)
        return (lambda x, *d: x.permute(*d))(o, 0, 2, 1, 3).reshape(B, Sq, H * Dh) @ wo

    def _B_ref(x, k_cache, v_cache, wq, wo):
        B, Sq, D = x.shape
        Hq, Dh = 16, 256
        Hkv = k_cache.shape[1]
        rep = Hq // Hkv
        q = (lambda x, *d: x.permute(*d))((x @ wq).reshape(B, Sq, Hq, Dh), 0, 2, 1, 3)
        k = (lambda x, n, axis: torch.repeat_interleave(x, n, dim=axis))(k_cache, rep, axis=1)
        v = (lambda x, n, axis: torch.repeat_interleave(x, n, dim=axis))(v_cache, rep, axis=1)
        scores = torch.einsum("bhqd,bhkd->bhqk", q, k) * (Dh ** -0.5)
        p = _softmax(scores, axis=-1)
        o = torch.einsum("bhqk,bhkd->bhqd", p, v)
        return (lambda x, *d: x.permute(*d))(o, 0, 2, 1, 3).reshape(B, Sq, Hq * Dh) @ wo

    a = _A_ref(A_x, A_k_cache, A_v_cache, A_wq, A_wo)
    b = _B_ref(B_x, B_k_cache, B_v_cache, B_wq, B_wo)
    return torch.concatenate([a, b], axis=-1)
```

```cpp
#include <hip/hip_runtime.h>
#include <hip/hip_bf16.h>
#include <stdint.h>

typedef unsigned short u16;
__device__ __forceinline__ float bf2f(u16 v){ return __uint_as_float(((unsigned)v)<<16); }
__device__ __forceinline__ u16 f2bf(float f){ __hip_bfloat16 b = __float2bfloat16(f); return *reinterpret_cast<u16*>(&b); }

constexpr int B=8, SQ=96, D=448, H=16, SK=512, DH=256;

__global__ void k_qproj(const u16* xa, const u16* wa, const u16* xb, const u16* wb, u16* q){
  int br = blockIdx.z; const u16* x = br? xb: xa; const u16* w = br? wb: wa;
  int row = blockIdx.y; int col = blockIdx.x*256+threadIdx.x;
  float acc=0;
  for(int k=0;k<D;k++) acc += bf2f(x[row*D+k])*bf2f(w[(size_t)k*4096+col]);
  q[((size_t)br*768+row)*4096+col]=f2bf(acc);
}
__global__ void k_attn(const u16* q, const u16* ka, const u16* va, const u16* kb, const u16* vb, u16* o){
  int br = blockIdx.z; const u16* kc = br? kb: ka; const u16* vc = br? vb: va;
  int bh = blockIdx.y; int b = bh/16, h = bh%16; int s = blockIdx.x;
  __shared__ float qs[256]; __shared__ float p[512]; __shared__ float red[256];
  int t = threadIdx.x;
  qs[t] = bf2f(q[((size_t)br*768+b*96+s)*4096+h*256+t]);
  __syncthreads();
  const u16* kp = kc + (size_t)bh*SK*DH; const u16* vp = vc + (size_t)bh*SK*DH;
  float mx=-1e30f;
  for(int j=t;j<512;j+=256){ float a=0; for(int d=0;d<256;d++) a+=qs[d]*bf2f(kp[j*256+d]); a = bf2f(f2bf(a))*0.0625f; a=bf2f(f2bf(a)); p[j]=a; mx=fmaxf(mx,a);}
  red[t]=mx; __syncthreads();
  for(int o_=128;o_>0;o_>>=1){ if(t<o_) red[t]=fmaxf(red[t],red[t+o_]); __syncthreads(); }
  mx=red[0]; __syncthreads();
  float sm=0; for(int j=t;j<512;j+=256){ float e=__expf(p[j]-mx); p[j]=e; sm+=e;}
  red[t]=sm; __syncthreads();
  for(int o_=128;o_>0;o_>>=1){ if(t<o_) red[t]+=red[t+o_]; __syncthreads(); }
  sm=red[0]; __syncthreads();
  for(int j=t;j<512;j+=256) p[j]=bf2f(f2bf(p[j]/sm));
  __syncthreads();
  float a=0; for(int j=0;j<512;j++) a+=p[j]*bf2f(vp[j*256+t]);
  o[((size_t)br*768+b*96+s)*4096+h*256+t]=f2bf(a);
}
__global__ void k_oproj(const u16* o, const u16* wa, const u16* wb, u16* out){
  int br = blockIdx.z; const u16* w = br? wb: wa;
  int row = blockIdx.y; int col = blockIdx.x*64+threadIdx.x;
  float acc=0;
  const u16* op = o+((size_t)br*768+row)*4096;
  for(int k=0;k<4096;k++) acc += bf2f(op[k])*bf2f(w[(size_t)k*448+col]);
  out[(size_t)row*896+br*448+col]=f2bf(acc);
}

extern "C" void kernel_launch(void* const* d_in, const int* in_sizes, int n_in,
                              void* d_out, int out_size, void* d_ws, size_t ws_size,
                              hipStream_t stream) {
  const u16* ax=(const u16*)d_in[0]; const u16* ak=(const u16*)d_in[1]; const u16* av=(const u16*)d_in[2];
  const u16* awq=(const u16*)d_in[3]; const u16* awo=(const u16*)d_in[4];
  const u16* bx=(const u16*)d_in[5]; const u16* bk=(const u16*)d_in[6]; const u16* bv=(const u16*)d_in[7];
  const u16* bwq=(const u16*)d_in[8]; const u16* bwo=(const u16*)d_in[9];
  u16* q=(u16*)d_ws; u16* o=q+(size_t)2*768*4096;
  k_qproj<<<dim3(16,768,2),256,0,stream>>>(ax,awq,bx,bwq,q);
  k_attn<<<dim3(96,128,2),256,0,stream>>>(q,ak,av,bk,bv,o);
  k_oproj<<<dim3(7,768,2),64,0,stream>>>(o,awo,bwo,(u16*)d_out);
}
```

```cpp
#include <hip/hip_runtime.h>
#include <stdint.h>
#include <utility>

typedef unsigned short u16;
typedef float f32x16 __attribute__((ext_vector_type(16)));
typedef float f32x4 __attribute__((ext_vector_type(4)));
typedef float f32x2 __attribute__((ext_vector_type(2)));
typedef __bf16 bf16x8 __attribute__((ext_vector_type(8)));
typedef __bf16 bf16x2 __attribute__((ext_vector_type(2)));
typedef short s16x4 __attribute__((ext_vector_type(4)));
typedef short s16x8 __attribute__((ext_vector_type(8)));
typedef unsigned u32x4 __attribute__((ext_vector_type(4)));
typedef unsigned u32x2 __attribute__((ext_vector_type(2)));
#define LAS __attribute__((address_space(3)))
#define DI __device__ __forceinline__

#define POL_KV " nt"
#define POL_W " nt"
#define POL_X " nt"

__device__ const unsigned kSeq[64] = {0,1,2,3,4,5,6,7,8,9,10,11,12,13,14,15,16,17,18,19,20,21,22,23,24,25,26,27,28,29,30,31,
                                      32,33,34,35,36,37,38,39,40,41,42,43,44,45,46,47,48,49,50,51,52,53,54,55,56,57,58,59,60,61,62,63};
#define POLT " sc1"
#define POLWT ""
#define POLOUT ""
#define NKQ 9
#define PARK 0
#define KSCHED 0100000020
#define CODE_BYTES 65536
#define WTEARLY 2
#define NOTOUCH 1
#define PUBLATE 0
#define SPACES 0
#define SPACEQ 0
#define SPACEV 0
#define T1FIRST 1
#define CODENT false
#define CODESCALAR 0
#define KGATE 17
constexpr unsigned SLOT = 16384u;
constexpr unsigned LDS_BYTES = 163840u;
constexpr unsigned FLAG_OFF = LDS_BYTES - 256u;
constexpr unsigned CONS_OFF = LDS_BYTES - 2048u;
constexpr unsigned CONSP_OFF = LDS_BYTES - 3072u;
constexpr unsigned DUMMY_OFF = 152576u;
constexpr unsigned POLL_OFF = 153600u;
constexpr unsigned X_OFF = 4u * SLOT;
constexpr size_t WS_E = 0;
constexpr size_t WS_FO = 4096;
constexpr size_t WS_FT = 262144;
constexpr size_t O_OFF = 1u << 20;
constexpr size_t T_OFF = 16u << 20;

constexpr int idxW(int i) { return i < 4 ? i : i < 6 ? i + 1 : i < 10 ? i + 3 : i + 5; }
constexpr int idxK(int m) { return m == 0 ? 4 : m < 3 ? m + 6 : m < 5 ? m + 10 : m + 14; }
#define ORDER 4
constexpr int kOrd[32] = {0,1,2,3,4,5,6,7,8,9, 16, 10, 17, 11, 20, 12, 21, 13, 22, 14, 18, 15, 23, 19, 24, 25, 26,27,28, 29,30,31};
constexpr int kTS[4] = {4, 8, 14, 22}, kTF[4] = {6, 10, 16, 23}, kSF[4] = {13, 13, 13, 13};
#define WT {9, 9, 9, 9}
constexpr int kWT[4] = WT;
constexpr int oPos(int i) { int p = 0; for (int j = 0; j < i; j++) p += kOrd[j] >= 20 ? 2 : 1; return p + (kOrd[i] >= 20 ? 1 : 0); }
constexpr int oFirstOP() { for (int i = 0; i < 32; i++) if (kOrd[i] >= 16) return i; return 32; }
constexpr int oIndex(int code) { for (int i = 0; i < 32; i++) if (kOrd[i] == code) return i; return -1; }
constexpr int tStep(int t, int s) {
  for (int c = 0; c < 4; c++) if ((s == 0 ? kTS[c] : kTF[c]) == t) return c;
  return -1;
}
constexpr int vmBetween(int a, int b) {
  int n = 0;
  for (int c = 0; c < 4; c++) {
    for (int u = 0; u < 4; u++) { const int x = 16 * kTS[c] + 3 + 2 * u; n += (x > a && x < b); }
    { const int x = 16 * kTF[c] + kSF[c]; n += (x > a && x < b) ? 1 : 0; }
  }
  return n;
}
static_assert(kTS[0] > oIndex(3) && kTS[1] > oIndex(7) && kTS[2] > oIndex(11) && kTS[3] > oIndex(15), "a chunk is stored after its last V tile");
static_assert(oIndex(16) > kTS[0] && oIndex(17) > kTS[1] && oIndex(18) > kTS[2] && oIndex(19) > kTS[3], "own-head tiles follow the chunk's epilogue");
template <int... Is, class F> DI void sfor_(std::integer_sequence<int, Is...>, F&& f) { (f(std::integral_constant<int, Is>{}), ...); }
template <int N, class F> DI void sfor(F&& f) { sfor_(std::make_integer_sequence<int, N>{}, f); }

DI unsigned rfl(unsigned x) { return __builtin_amdgcn_readfirstlane(x); }
DI unsigned cvtpk(float lo, float hi) { f32x2 v = {lo, hi}; bf16x2 b = __builtin_convertvector(v, bf16x2); return __builtin_bit_cast(unsigned, b); }
DI bf16x8 pack8(const f32x16& a, int base) {
  u32x4 w = {cvtpk(a[base + 0], a[base + 1]), cvtpk(a[base + 2], a[base + 3]), cvtpk(a[base + 4], a[base + 5]), cvtpk(a[base + 6], a[base + 7])};
  return __builtin_bit_cast(bf16x8, w);
}
DI bf16x8 trpair(LAS char* p0, LAS char* p1) {
  s16x4 a = __builtin_amdgcn_ds_read_tr16_b64_v4i16((LAS s16x4*)p0);
  s16x4 b = __builtin_amdgcn_ds_read_tr16_b64_v4i16((LAS s16x4*)p1);
  s16x8 r = __builtin_shufflevector(a, b, 0, 1, 2, 3, 4, 5, 6, 7);
  return __builtin_bit_cast(bf16x8, r);
}
#define ACLOB "a0","a1","a2","a3","a4","a5","a6","a7","a8","a9","a10","a11","a12","a13","a14","a15","a16","a17","a18","a19","a20","a21","a22","a23","a24","a25","a26","a27","a28","a29","a30","a31","a32","a33","a34","a35","a36","a37","a38","a39","a40","a41","a42","a43","a44","a45","a46","a47","a48","a49","a50","a51","a52","a53","a54","a55","a56","a57","a58","a59","a60","a61","a62","a63","a64","a65","a66","a67","a68","a69","a70","a71","a72","a73","a74","a75","a76","a77","a78","a79","a80","a81","a82","a83","a84","a85","a86","a87","a88","a89","a90","a91","a92","a93","a94","a95","a96","a97","a98","a99","a100","a101","a102","a103","a104","a105","a106","a107","a108","a109","a110","a111","a112","a113","a114","a115","a116","a117","a118","a119","a120","a121","a122","a123","a124","a125","a126","a127","a128","a129","a130","a131","a132","a133","a134","a135","a136","a137","a138","a139","a140","a141","a142","a143","a144","a145","a146","a147","a148","a149","a150","a151","a152","a153","a154","a155","a156","a157","a158","a159","a160","a161","a162","a163","a164","a165","a166","a167","a168","a169","a170","a171","a172","a173","a174","a175","a176","a177","a178","a179","a180","a181","a182","a183","a184","a185","a186","a187","a188","a189","a190","a191","a192","a193","a194","a195","a196","a197","a198","a199","a200","a201","a202","a203","a204","a205","a206","a207","a208","a209","a210","a211","a212","a213","a214","a215","a216","a217","a218","a219","a220","a221","a222","a223","a224","a225","a226","a227","a228","a229","a230","a231","a232","a233","a234","a235","a236","a237","a238","a239","a240","a241","a242","a243","a244","a245","a246","a247","a248","a249","a250","a251","a252","a253","a254","a255"
template <int A, bool ZERO> DI void mfma_a(const bf16x8& a, const bf16x8& b) {
  if constexpr (ZERO) asm volatile("v_mfma_f32_32x32x16_bf16 a[%c0:%c1], %2, %3, 0" :: "i"(A), "i"(A + 15), "v"(a), "v"(b) : ACLOB);
  else                asm volatile("v_mfma_f32_32x32x16_bf16 a[%c0:%c1], %2, %3, a[%c0:%c1]" :: "i"(A), "i"(A + 15), "v"(a), "v"(b) : ACLOB);
}
template <int B, bool ZERO> DI void mfma_vb(f32x16& acc, const bf16x8& a) {
  if constexpr (ZERO) asm volatile("v_mfma_f32_32x32x16_bf16 %0, %1, a[%c2:%c2+3], 0" : "=&v"(acc) : "v"(a), "i"(B));
  else                asm volatile("v_mfma_f32_32x32x16_bf16 %0, %1, a[%c2:%c2+3], %0" : "+v"(acc) : "v"(a), "i"(B));
}
template <int A, int B, bool ZERO> DI void mfma_ab(const bf16x8& a) {
  if constexpr (ZERO) asm volatile("v_mfma_f32_32x32x16_bf16 a[%c0:%c0+15], %1, a[%c2:%c2+3], 0" :: "i"(A), "v"(a), "i"(B) : ACLOB);
  else                asm volatile("v_mfma_f32_32x32x16_bf16 a[%c0:%c0+15], %1, a[%c2:%c2+3], a[%c0:%c0+15]" :: "i"(A), "v"(a), "i"(B) : ACLOB);
}
template <int A> DI void ofrag4(unsigned voff, __amdgpu_buffer_rsrc_t srd, unsigned soff) {
  asm volatile("buffer_load_dwordx4 a[%c0:%c0+3], %1, %2, %3 offen sc1\n\tbuffer_load_dwordx4 a[%c0+4:%c0+7], %1, %2, %3 offen offset:1024 sc1\n\t"
               "buffer_load_dwordx4 a[%c0+8:%c0+11], %1, %2, %3 offen offset:2048 sc1\n\tbuffer_load_dwordx4 a[%c0+12:%c0+15], %1, %2, %3 offen offset:3072 sc1"
               :: "i"(A), "v"(voff), "s"(srd), "s"(soff) : "memory", ACLOB);
}
template <int A, int O> DI void ofrag1(unsigned voff, __amdgpu_buffer_rsrc_t srd, unsigned soff) {
  asm volatile("buffer_load_dwordx4 a[%c0:%c0+3], %1, %2, %3 offen offset:%c4 sc1" :: "i"(A), "v"(voff), "s"(srd), "s"(soff), "i"(O) : "memory", ACLOB);
}
template <int A> DI void acc_wr4(const u32x4& w) {
  asm volatile("v_accvgpr_write_b32 a[%c4], %0\n\tv_accvgpr_write_b32 a[%c4+1], %1\n\tv_accvgpr_write_b32 a[%c4+2], %2\n\tv_accvgpr_write_b32 a[%c4+3], %3"
               :: "v"(w[0]), "v"(w[1]), "v"(w[2]), "v"(w[3]), "i"(A) : ACLOB);
}
template <int A> DI f32x16 acc_rd16() {
  f32x16 r;
  float r0, r1, r2, r3, r4, r5, r6, r7, r8, r9, r10, r11, r12, r13, r14, r15;
  asm volatile("v_accvgpr_read_b32 %0, a[%c16]\n\tv_accvgpr_read_b32 %1, a[%c16+1]\n\tv_accvgpr_read_b32 %2, a[%c16+2]\n\tv_accvgpr_read_b32 %3, a[%c16+3]\n\t"
               "v_accvgpr_read_b32 %4, a[%c16+4]\n\tv_accvgpr_read_b32 %5, a[%c16+5]\n\tv_accvgpr_read_b32 %6, a[%c16+6]\n\tv_accvgpr_read_b32 %7, a[%c16+7]\n\t"
               "v_accvgpr_read_b32 %8, a[%c16+8]\n\tv_accvgpr_read_b32 %9, a[%c16+9]\n\tv_accvgpr_read_b32 %10, a[%c16+10]\n\tv_accvgpr_read_b32 %11, a[%c16+11]\n\t"
               "v_accvgpr_read_b32 %12, a[%c16+12]\n\tv_accvgpr_read_b32 %13, a[%c16+13]\n\tv_accvgpr_read_b32 %14, a[%c16+14]\n\tv_accvgpr_read_b32 %15, a[%c16+15]"
               : "=v"(r0), "=v"(r1), "=v"(r2), "=v"(r3), "=v"(r4), "=v"(r5), "=v"(r6), "=v"(r7), "=v"(r8), "=v"(r9), "=v"(r10), "=v"(r11), "=v"(r12), "=v"(r13), "=v"(r14), "=v"(r15)
               : "i"(A));
  r[0] = r0; r[1] = r1; r[2] = r2; r[3] = r3; r[4] = r4; r[5] = r5; r[6] = r6; r[7] = r7;
  r[8] = r8; r[9] = r9; r[10] = r10; r[11] = r11; r[12] = r12; r[13] = r13; r[14] = r14; r[15] = r15;
  return r;
}
#define MFMA_DRAIN() asm volatile("s_nop 15\n\ts_nop 3" ::: "memory")
#define SB() __builtin_amdgcn_sched_barrier(0)

#define POL " nt"
#define POLX ""
DI void dma1(unsigned ldsaddr, unsigned voff, __amdgpu_buffer_rsrc_t srd, unsigned soff) {
  asm volatile("s_mov_b32 m0, %0\n\ts_nop 0\n\tbuffer_load_dwordx4 %1, %2, %3 offen" POL " lds" :: "s"(ldsaddr), "v"(voff), "s"(srd), "s"(soff) : "memory");
}
#define POLW " nt"
DI void dma4w(unsigned ldsaddr, unsigned v0, unsigned v1, unsigned v2, unsigned v3, __amdgpu_buffer_rsrc_t srd, unsigned soff) {
  asm volatile("s_mov_b32 m0, %0\n\ts_nop 0\n\tbuffer_load_dwordx4 %1, %5, %6 offen" POLW " lds\n\tbuffer_load_dwordx4 %2, %5, %6 offen offset:1024" POLW " lds\n\t"
               "buffer_load_dwordx4 %3, %5, %6 offen offset:2048" POLW " lds\n\tbuffer_load_dwordx4 %4, %5, %6 offen offset:3072" POLW " lds"
               :: "s"(ldsaddr), "v"(v0), "v"(v1), "v"(v2), "v"(v3), "s"(srd), "s"(soff) : "memory");
}
DI void dma4(unsigned ldsaddr, unsigned v0, unsigned v1, unsigned v2, unsigned v3, __amdgpu_buffer_rsrc_t srd, unsigned soff) {
  asm volatile("s_mov_b32 m0, %0\n\ts_nop 0\n\tbuffer_load_dwordx4 %1, %5, %6 offen" POL " lds\n\tbuffer_load_dwordx4 %2, %5, %6 offen offset:1024" POL " lds\n\t"
               "buffer_load_dwordx4 %3, %5, %6 offen offset:2048" POL " lds\n\tbuffer_load_dwordx4 %4, %5, %6 offen offset:3072" POL " lds"
               :: "s"(ldsaddr), "v"(v0), "v"(v1), "v"(v2), "v"(v3), "s"(srd), "s"(soff) : "memory");
}
#define POLO " sc1"
#define ACQ 0
DI void dma4s(unsigned ldsaddr, unsigned v0, __amdgpu_buffer_rsrc_t srd, unsigned soff) {
  asm volatile("s_mov_b32 m0, %0\n\ts_nop 0\n\tbuffer_load_dwordx4 %1, %2, %3 offen" POLO " lds\n\tbuffer_load_dwordx4 %1, %2, %3 offen offset:1024" POLO " lds\n\t"
               "buffer_load_dwordx4 %1, %2, %3 offen offset:2048" POLO " lds\n\tbuffer_load_dwordx4 %1, %2, %3 offen offset:3072" POLO " lds"
               :: "s"(ldsaddr), "v"(v0), "s"(srd), "s"(soff) : "memory");
}
DI void dma3s(unsigned ldsaddr, unsigned v0, __amdgpu_buffer_rsrc_t srd, unsigned soff) {
  asm volatile("s_mov_b32 m0, %0\n\ts_nop 0\n\tbuffer_load_dwordx4 %1, %2, %3 offen sc1 lds\n\tbuffer_load_dwordx4 %1, %2, %3 offen offset:1024 sc1 lds\n\t"
               "buffer_load_dwordx4 %1, %2, %3 offen offset:2048 sc1 lds"
               :: "s"(ldsaddr), "v"(v0), "s"(srd), "s"(soff) : "memory");
}
#define DMA1(pol, a, b, c, d) dma1(a, b, c, d)
#define DMA4(pol, a, b, c, d, e, f, g) dma4(a, b, c, d, e, f, g)

DI void touch_buf(unsigned dummy, unsigned voff, __amdgpu_buffer_rsrc_t srd, unsigned soff, bool nt) {
  if (nt) asm volatile("s_mov_b32 m0, %0\n\ts_nop 0\n\tbuffer_load_dword %1, %2, %3 offen nt lds" :: "s"(dummy), "v"(voff), "s"(srd), "s"(soff) : "memory");
  else    asm volatile("s_mov_b32 m0, %0\n\ts_nop 0\n\tbuffer_load_dword %1, %2, %3 offen lds" :: "s"(dummy), "v"(voff), "s"(srd), "s"(soff) : "memory");
}
DI void poll_dma(unsigned ldsaddr, unsigned voff, __amdgpu_buffer_rsrc_t srd) {
  asm volatile("s_mov_b32 m0, %0\n\ts_nop 0\n\tbuffer_load_dwordx4 %1, %2, 0 offen sc1 lds" :: "s"(ldsaddr), "v"(voff), "s"(srd) : "memory");
}
DI void dma_flag(unsigned ldsaddr, __amdgpu_buffer_rsrc_t srd, unsigned val) {
  asm volatile("s_mov_b32 m0, %0\n\ts_mov_b64 exec, 1\n\tbuffer_load_dword %1, %2, %3 offen lds\n\ts_mov_b64 exec, -1" :: "s"(ldsaddr), "v"(0u), "s"(srd), "s"(val * 4u) : "memory");
}
template <int A> DI void park4(unsigned v0, unsigned v1, unsigned v2, unsigned v3, __amdgpu_buffer_rsrc_t srd, unsigned soff) {
  asm volatile("buffer_load_dwordx4 a[%c0:%c0+3], %1, %5, %6 offen" POL "\n\tbuffer_load_dwordx4 a[%c0+4:%c0+7], %2, %5, %6 offen offset:1024" POL "\n\t"
               "buffer_load_dwordx4 a[%c0+8:%c0+11], %3, %5, %6 offen offset:2048" POL "\n\tbuffer_load_dwordx4 a[%c0+12:%c0+15], %4, %5, %6 offen offset:3072" POL
               :: "i"(A), "v"(v0), "v"(v1), "v"(v2), "v"(v3), "s"(srd), "s"(soff) : "memory");
}
template <int A, int O> DI void unpark4(unsigned addr) {
  asm volatile("ds_write_b128 %1, a[%c0:%c0+3] offset:%c2\n\tds_write_b128 %1, a[%c0+4:%c0+7] offset:%c2+1024\n\t"
               "ds_write_b128 %1, a[%c0+8:%c0+11] offset:%c2+2048\n\tds_write_b128 %1, a[%c0+12:%c0+15] offset:%c2+3072"
               :: "i"(A), "v"(addr), "i"(O) : "memory");
}
template <int N> DI void flagstN(char* p, unsigned long long tag) {
  asm volatile("s_waitcnt vmcnt(%2)\n\ts_mov_b64 exec, 1\n\tglobal_store_dwordx2 %0, %1, off sc1\n\ts_mov_b64 exec, -1" :: "v"(p), "v"(tag), "i"(N) : "memory");
}
DI unsigned lds_ld(LAS char* p) { return *(volatile LAS unsigned*)p; }
DI void lds_st1(unsigned addr, unsigned val) {
  asm volatile("s_mov_b64 exec, 1\n\tds_write_b32 %0, %1\n\ts_mov_b64 exec, -1" :: "v"(addr), "v"(val) : "memory");
}

#define DEPTH 30
#define DEPTH2 50
#define DEPTHO 16
#define ROOM() do {} while (0)
#define GATE(n) asm volatile("s_waitcnt vmcnt(%0)" :: "i"(n) : "memory")
#define STAMP(k) do {} while (0)
#define CST(k) do {} while (0)

extern "C" __global__ __launch_bounds__(256, 1)
void fused_attn(const u16* __restrict__ xA, const u16* __restrict__ kA, const u16* __restrict__ vA, const u16* __restrict__ wqA,
                const u16* __restrict__ xB, const u16* __restrict__ kB, const u16* __restrict__ vB, const u16* __restrict__ wqB,
                const u16* __restrict__ woA, const u16* __restrict__ woB, u16* __restrict__ out, char* __restrict__ ws) {
  extern __shared__ __attribute__((aligned(16))) char lds_raw[];
  LAS char* lds = (LAS char*)lds_raw;
  const unsigned ldsb = (unsigned)(size_t)lds;
  const int tid = threadIdx.x;
  const int wave = rfl(tid >> 6);
  const unsigned lane = tid & 63;
  const int wg = blockIdx.x;
  const int xg = wg & 7, idx = wg >> 3;
  const int br = xg >> 2, jk = xg & 3, b = idx >> 2, jn = idx & 3, h = 4 * jk + jn;
  const u16* xp = br ? xB : xA; const u16* kp = br ? kB : kA; const u16* vp = br ? vB : vA; const u16* wqp = br ? wqB : wqA; const u16* wop = br ? woB : woA;
  const size_t bh = (size_t)(b * 16 + h);
  asm volatile("" ::: "a255");
  unsigned long long pcTop;
  asm volatile("s_getpc_b64 %0" : "=s"(pcTop));
  STAMP(0);
  const __amdgpu_buffer_rsrc_t srdK = __builtin_amdgcn_make_buffer_rsrc((void*)(kp + bh * 131072), (short)0, 262144, 0x00020000);
  const unsigned hi = lane >> 5, l31 = lane & 31;
  const unsigned voffK0 = hi * 512u + ((l31 ^ hi) << 4);

  unsigned long long tag = 0;
  auto read_tag = [&]() {
    const char* ep = ws + WS_E + (br * 8 + b) * 128;
    asm volatile("s_load_dwordx2 %0, %1, 0x0\n\ts_waitcnt lgkmcnt(0)" : "=s"(tag) : "s"(ep) : "memory");
    tag = tag * 6364136223846793005ull + 1442695040888963407ull;
  };
  const int wgi = (br * 8 + b) * 16 + h;
  const unsigned lane16 = lane * 16u;
  auto poll = [&](const char* p) {
#pragma unroll 1
    for (int spin = 0; spin < (1 << 18); spin++) {
      unsigned long long v;
      asm volatile("global_load_dwordx2 %0, %1, off sc1\n\ts_waitcnt vmcnt(0)" : "=&v"(v) : "v"(p) : "memory");
      if (__all(v == tag)) break;
    }
  };
  const __amdgpu_buffer_rsrc_t srdS = __builtin_amdgcn_make_buffer_rsrc((void*)kSeq, (short)0, 256, 0x00020000);
  const __amdgpu_buffer_rsrc_t srdO = __builtin_amdgcn_make_buffer_rsrc((void*)(wop + (size_t)jk * 1024 * 448 + jn * 128), (short)0, (4 - jk) * 1024 * 896 - jn * 256, 0x00020000);
  const __amdgpu_buffer_rsrc_t srdOt = __builtin_amdgcn_make_buffer_rsrc((void*)(wop + (size_t)jk * 1024 * 448), (short)0, 1024 * 896, 0x00020000);
  const __amdgpu_buffer_rsrc_t srdOr = __builtin_amdgcn_make_buffer_rsrc((void*)(ws + O_OFF + (size_t)(wgi - jn) * 49152), (short)0, 4 * 49152, 0x00020000);
  const __amdgpu_buffer_rsrc_t srdOw = __builtin_amdgcn_make_buffer_rsrc((void*)(ws + O_OFF + (size_t)wgi * 49152), (short)0, 49152, 0x00020000);
  unsigned voffO[4] = {0u, 0u, 0u, 0u}, voOt = 0;
  char* foMine = ws + WS_FO + ((size_t)wgi * 12 + (wave < 3 ? wave : 0)) * 64;
  const char* foPoll = ws + WS_FO + ((size_t)(wgi - jn + ((lane >> 2) & 3)) * 12 + ((lane & 3) < 3 ? (lane & 3) : 0)) * 64;
  auto touchWo = [&](unsigned c) { asm volatile("s_mov_b32 m0, %0\n\ts_nop 0\n\tbuffer_load_dword %1, %2, %3 offen" POLWT " lds" :: "s"(ldsb + DUMMY_OFF), "v"(voOt), "s"(srdOt), "s"(c * 64u * 896u) : "memory"); };

  if (wave == 3) {
    const __amdgpu_buffer_rsrc_t srdV = __builtin_amdgcn_make_buffer_rsrc((void*)(vp + bh * 131072), (short)0, 262144, 0x00020000);
    const __amdgpu_buffer_rsrc_t srdW = __builtin_amdgcn_make_buffer_rsrc((void*)(wqp + h * 256), (short)0, 448 * 8192 - h * 512, 0x00020000);
    const unsigned voT = (b + 8u * (lane >> 2)) * 8192u + (lane & 3) * 128u;
    unsigned voffW[4], voffK[8], voffV[4];
#pragma unroll
    for (int e = 0; e < 4; e++) voffW[e] = hi * 8192u + ((l31 ^ (((2u * e + hi) & 3u) << 2)) << 4) + e * 15360u;
#pragma unroll
    for (int e = 0; e < 8; e++) voffK[e] = voffK0 ^ (32u * e);
#pragma unroll
    for (int e = 0; e < 4; e++) voffV[e] = (lane >> 3) * 512u + (((lane & 7) ^ (((lane >> 4) & 1) << 2)) << 4) + e * 3072u;
    auto issueW = [&](unsigned i) {
      const unsigned la = ldsb + ((i + 2) & 3) * SLOT, so = i * 32u * 8192u;
#pragma unroll
      for (int q = 0; q < 4; q++) { GATE(DEPTH); dma4w(la + q * 4096u, voffW[0], voffW[1], voffW[2], voffW[3], srdW, so + q * 65536u); }
    };
    unsigned slotU = 4;
    auto issueK = [&](unsigned m) {
      const unsigned la = ldsb + slotU * SLOT, so = m * 16384u;
#pragma unroll
      for (int q = 0; q < 4; q++) { GATE(DEPTH); dma4(la + q * 4096u, voffK[(4 * q) & 7], voffK[(4 * q + 1) & 7], voffK[(4 * q + 2) & 7], voffK[(4 * q + 3) & 7], srdK, so + q * 4096u); }
      slotU = slotU == 8 ? 0 : slotU + 1;
    };
    auto issueV = [&](unsigned v) {
      const unsigned la = ldsb + slotU * SLOT, so = (v & 3) * 65536u + (v >> 2) * 128u;
#pragma unroll
      for (int q = 0; q < 4; q++) { GATE(DEPTH); dma4(la + q * 4096u, voffV[0], voffV[1], voffV[2], voffV[3], srdV, so + q * 16384u); }
      slotU = slotU == 8 ? 0 : slotU + 1;
    };
    const unsigned flW = ldsb + FLAG_OFF, flU = ldsb + FLAG_OFF + 4;
    auto cons_min = [&]() { const u32x4 c = *(volatile LAS u32x4*)(lds + CONS_OFF); return rfl(min(min(c[0], c[1]), c[2])); };

    *(volatile LAS u32x4*)(lds + CONS_OFF + lane * 16) = u32x4{0u, 0u, 0u, 0u};
    *(volatile LAS u32x4*)(lds + CONSP_OFF + lane * 16) = u32x4{0u, 0u, 0u, 0u};
    if (lane < 16) *(volatile LAS u32x4*)(lds + FLAG_OFF + lane * 16) = u32x4{0u, 0u, 0u, 0u};
    asm volatile("s_waitcnt lgkmcnt(0)" ::: "memory");
    touch_buf(ldsb + DUMMY_OFF, voT, srdW, 0u, true);
    read_tag();
    asm volatile("s_barrier" ::: "memory");
    STAMP(12);
    issueW(0); dma_flag(flW, srdS, 1);
    issueW(1); dma_flag(flW, srdS, 2);
    issueW(2); dma_flag(flW, srdS, 3);
    ROOM();
    issueW(3); dma_flag(flW, srdS, 4);
    ROOM();
    {
      const __amdgpu_buffer_rsrc_t srdC = __builtin_amdgcn_make_buffer_rsrc((void*)(pcTop & ~255ull), (short)0, CODE_BYTES, 0x00020000);
      touch_buf(ldsb + DUMMY_OFF, lane < (CODE_BYTES / 128 + 31) / 32 ? ((unsigned)idx * ((CODE_BYTES / 128 + 31) / 32) + lane) * 128u : 0x7fff0000u, srdC, 0u, CODENT);
    }
    touch_buf(ldsb + DUMMY_OFF, lane == 0 ? 0u : lane == 1 ? 262144u - 128u : 0x7fff0000u, srdV, 0u, true);
    touch_buf(ldsb + DUMMY_OFF, voT, srdW, 128u * 8192u, true); touch_buf(ldsb + DUMMY_OFF, voT, srdW, 256u * 8192u, true); touch_buf(ldsb + DUMMY_OFF, voT, srdW, 384u * 8192u, true);
    STAMP(1);
    { u32x4 c; do { c = *(volatile LAS u32x4*)(lds + FLAG_OFF + 48); } while (rfl(c[0] & c[1] & c[2]) == 0); }
    unsigned nW = 4, nU = 0;
    auto issueNextK = [&]() { issueK(nU); nU++; dma_flag(flU, srdS, nU); };
    unsigned c = 0;
    sfor<10>([&](auto N) {
      constexpr int n = 4 + N;
      constexpr int nk = (KSCHED >> (3 * N)) & 7;
      sfor<nk>([&](auto) { ROOM(); issueNextK(); });
      while (c + 3 < (unsigned)n) c = cons_min();
      ROOM();
      issueW(n); dma_flag(flW, srdS, n + 1);
    });
    STAMP(2);
#pragma unroll 1
    for (; nU < 16; nU++) { if (nU >= 5) { while (cons_min() < nU + 6) {} } ROOM(); issueK(nU); dma_flag(flU, srdS, nU + 1); }
    STAMP(3);
    voOt = lane < 56 ? ((unsigned)(idx >> 3) * 256u + 8u * (idx & 7) + (lane * 37u >> 8)) * 896u + (lane - 7u * (lane * 37u >> 8)) * 128u : 0x7fffff00u;
#pragma unroll
    for (int e = 0; e < 4; e++) {
      const unsigned pc = (lane & 15) ^ (((lane >> 4) & 3) << 2);
      voffO[e] = pc < (jn == 3 ? 8u : 16u) ? (lane >> 4) * 896u + (pc << 4) + e * 2560u : 0x7fff0000u;
    }
    auto issueWo = [&](unsigned c, unsigned eta) {
      const unsigned la = ldsb + slotU * SLOT, so = (eta * 256u + 64u * c) * 896u;
#pragma unroll
      for (int q = 0; q < 4; q++) { GATE(DEPTH); dma4w(la + q * 4096u, voffO[0], voffO[1], voffO[2], voffO[3], srdO, so + q * 16u * 896u); }
      slotU = slotU == 8 ? 0 : slotU + 1;
    };
    const __amdgpu_buffer_rsrc_t srdF = __builtin_amdgcn_make_buffer_rsrc((void*)(ws + WS_FO + (size_t)(wgi - jn) * 768), (short)0, 4 * 768, 0x00020000);
    const unsigned voF = ((lane >> 2) & 3) * 768u + ((lane & 3) < 3 ? (lane & 3) : 0) * 64u;
    auto pollLooked = [&]() { const unsigned long long v = *(volatile LAS unsigned long long*)(lds + POLL_OFF + lane * 16); return __all(v == tag); };
    unsigned cc = 0;
    sfor<32>([&](auto I) {
      constexpr int i = I, code = kOrd[i], pos = oPos(i);
      constexpr int nxt = i + 1 < 32 ? kOrd[i + 1 < 32 ? i + 1 : 31] : -1;
      if constexpr (i == kWT[0]) touchWo(0);
      if constexpr (i == kWT[1]) touchWo(1);
      if constexpr (i == kWT[2]) touchWo(2);
      if constexpr (i == kWT[3]) touchWo(3);
      if constexpr (code >= 20 && (code - 20) % 3 == 0) {
        constexpr int c = (code - 20) / 3;
        constexpr int prevLoads = kOrd[i > 0 ? i - 1 : 0] >= 20 ? 29 : 17;
        asm volatile("s_waitcnt vmcnt(%0)" :: "i"(prevLoads) : "memory");
        if (!pollLooked()) poll(foPoll + c * 192);
      }
      if constexpr (nxt >= 20 && (nxt - 20) % 3 == 0) poll_dma(ldsb + POLL_OFF, voF + ((nxt - 20) / 3) * 192u, srdF);
      while (cc < (unsigned)(pos + 22)) cc = cons_min();
      if constexpr (code < 16) issueV(code);
      else if constexpr (code < 20) issueWo(code - 16, jn);
      else {
        constexpr int c = (code - 20) / 3, e = (code - 20) % 3;
        const unsigned eta = e + (e >= jn ? 1u : 0u);
        const unsigned la = ldsb + slotU * SLOT, so = eta * 49152u + c * 12288u;
        GATE(33);
        dma4s(la, lane16, srdOr, so); dma4s(la + 4096u, lane16, srdOr, so + 4096u); dma4s(la + 8192u, lane16, srdOr, so + 8192u);
        slotU = slotU == 8 ? 0 : slotU + 1;
        const unsigned lw = ldsb + slotU * SLOT, sw = (eta * 256u + 64u * c) * 896u;
#pragma unroll
        for (int q = 0; q < 4; q++) dma4w(lw + q * 4096u, voffO[0], voffO[1], voffO[2], voffO[3], srdO, sw + q * 16u * 896u);
        slotU = slotU == 8 ? 0 : slotU + 1;
      }
      dma_flag(flU, srdS, pos + 17);
      if constexpr (code == 3) STAMP(6);
      if constexpr (code == 7) STAMP(7);
      if constexpr (code == 11) STAMP(8);
      if constexpr (code == 15) STAMP(9);
      if constexpr (code == 19) STAMP(10);
    });
    asm volatile("s_waitcnt vmcnt(0)" ::: "memory");
    STAMP(4);
  } else {
    const int w = wave;
    const unsigned hh = hi;
    LAS char* flg = lds + FLAG_OFF;
    auto wbar = [&](int i) {
      lds_st1(ldsb + FLAG_OFF + 32 + 16 * i + 4 * w, 1);
      u32x4 c; do { c = *(volatile LAS u32x4*)(lds + FLAG_OFF + 32 + 16 * i); } while (rfl(c[0] & c[1] & c[2]) == 0);
    };
    {
      const __amdgpu_buffer_rsrc_t srdX = __builtin_amdgcn_make_buffer_rsrc((void*)(xp + (size_t)(b * 96 + 32 * w) * 448), (short)0, 32 * 896, 0x00020000);
      auto xrows = [&](unsigned q) {
        const unsigned f0 = (2u * q) & 7u;
        const unsigned v0 = (lane ^ f0) << 4, v1 = (lane ^ (f0 + 1u)) << 4;
        asm volatile("s_mov_b32 m0, %0\n\ts_mov_b64 exec, %5\n\tbuffer_load_dwordx4 %1, %3, %4 offen" POLX " lds\n\tbuffer_load_dwordx4 %1, %3, %4 offen offset:896" POLX " lds\n\t"
                     "buffer_load_dwordx4 %2, %3, %4 offen offset:1792" POLX " lds\n\tbuffer_load_dwordx4 %2, %3, %4 offen offset:2688" POLX " lds\n\ts_mov_b64 exec, -1"
                     :: "s"(ldsb + X_OFF + (32u * w + 4u * q) * 896u), "v"(v0), "v"(v1), "s"(srdX), "s"(4u * q * 896u), "s"(0x00ffffffffffffffull) : "memory");
      };
      xrows(2u * jn); xrows(2u * jn + 1u);
      asm volatile("s_waitcnt vmcnt(0)" ::: "memory");
      STAMP(12);
#pragma unroll
      for (int i = 2; i < 8; i++) xrows((2u * jn + i) & 7u);
    }
    read_tag();
    asm volatile("s_barrier" ::: "memory");
    asm volatile("s_waitcnt vmcnt(0)" ::: "memory");
    bf16x8 xf[28];
    {
      const unsigned r = 32u * w + l31;
      const unsigned a0 = X_OFF + r * 896u, sw = (l31 >> 1) & 7u;
#pragma unroll
      for (int s = 0; s < 28; s++) { const unsigned pz = 2u * s + hh; xf[s] = *(LAS bf16x8*)(lds + a0 + (((pz & 7u) ^ sw) << 4) + ((pz >> 3) << 7)); }
    }
    lds_st1(ldsb + FLAG_OFF + 48 + 4 * w, 1);
    STAMP(1);
    LAS char* consP = lds + CONS_OFF + 4 * w + 16 * lane;
    unsigned fs = 0;
    unsigned fu = 0;
    auto waitW = [&](unsigned need) { while (fs < need) fs = rfl(lds_ld(flg)); };
    auto waitU = [&](unsigned need) { while (fu < need) fu = rfl(lds_ld(flg + 4)); };
    auto consumed = [&](unsigned cnt) { *(volatile LAS unsigned*)consP = cnt; };

    const unsigned g = lane >> 4, dhalf = g & 1, q4 = (lane & 15) >> 2, p = lane & 3;
    const unsigned pi = ((p & 1) << 1) | (p >> 1);
    unsigned aW[4];
#pragma unroll
    for (int tl = 0; tl < 4; tl++) aW[tl] = (8u * hh + q4) * 512u + 64u * (tl ^ q4) + 16u * (2u * dhalf + (pi >> 1)) + 8u * (pi & 1);

    bf16x8 fr[8];
    auto ldW = [&](int i, int k) -> bf16x8 {
      const unsigned o = ((i + 2) & 3) * SLOT + (k >> 3) * 8192u + ((k >> 2) & 1) * 256u;
      return trpair(lds + aW[k & 3] + o, lds + aW[k & 3] + o + 2048u);
    };
    waitW(1);
#pragma unroll
    for (int k = 0; k < 4; k++) fr[k] = ldW(0, k);
    sfor<14 * 16>([&](auto SG) {
      constexpr int sg = SG, i = sg >> 4, k = sg & 15, n = sg + 4;
      SB();
      mfma_a<16 * (k & 7), (i == 0 && k < 8)>(fr[sg & 7], xf[2 * i + (k >> 3)]);
      if constexpr (SPACEQ > 0) asm volatile("s_nop %0" :: "i"(SPACEQ - 1));
      SB();
      if constexpr (k == 12 && i < 13) { waitW(i + 2); }
      if constexpr (n < 14 * 16) fr[n & 7] = ldW(n >> 4, n & 15);
      if constexpr (k == 11) consumed(i + 1);
    });
    SB();
    STAMP(2);
    MFMA_DRAIN();
    sfor<8>([&](auto T) { constexpr int t = T; const f32x16 q = acc_rd16<16 * t>();
      acc_wr4<128 + 8 * t>(__builtin_bit_cast(u32x4, pack8(q, 0))); acc_wr4<128 + 8 * t + 4>(__builtin_bit_cast(u32x4, pack8(q, 8))); SB(); });
    waitU(1);
    STAMP(3);
    unsigned aK[3][8];
#pragma unroll
    for (int gq = 0; gq < 3; gq++)
#pragma unroll
      for (int e = 0; e < 8; e++) { aK[gq][e] = ((l31 * 512u + (((l31 & 15) ^ hh) << 4)) ^ (32u * e)) + gq * 65536u; asm volatile("" : "+v"(aK[gq][e])); }
    auto ldK = [&](int m, int k) -> bf16x8 { const int s = (m + 4) % 9; return *(LAS bf16x8*)(lds + aK[s >> 2][k & 7] + (s & 3) * SLOT + (k >> 3) * 256u); };
    float mx = -__builtin_inff();
    u32x4 Sp[32];
    f32x16 sacc[2];
#pragma unroll
    for (int k = 0; k < 4; k++) fr[k] = ldK(0, k);
    sfor<16 * 16>([&](auto SG) {
      constexpr int sg = SG, m = sg >> 4, k = sg & 15, n = sg + 4;
      SB();
      mfma_vb<128 + 4 * k, (k == 0)>(sacc[m & 1], fr[sg & 7]);
      if constexpr (SPACES > 0) asm volatile("s_nop %0" :: "i"(SPACES - 1));
      SB();
      if constexpr (k == 12 && m < 15) { waitU(m + 2); }
      if constexpr (n < 256) fr[n & 7] = ldK(n >> 4, n & 15);
      if constexpr (k == 11) consumed(14 + m + 1);
      if constexpr (m > 0 && k >= 3 && k < 11) {
        constexpr int j = k - 3;
        const float s0 = sacc[(m - 1) & 1][2 * j], s1 = sacc[(m - 1) & 1][2 * j + 1];
        mx = fmaxf(mx, fmaxf(s0, s1));
        Sp[2 * (m - 1) + (j >> 2)][j & 3] = cvtpk(s0, s1);
        asm volatile("" : "+v"(mx), "+v"(Sp[2 * (m - 1) + (j >> 2)]));
      }
    });
    SB();
    asm volatile("s_nop 15\n\ts_nop 3" : "+v"(sacc[1]));
#pragma unroll
    for (int j = 0; j < 8; j++) { const float s0 = sacc[1][2 * j], s1 = sacc[1][2 * j + 1]; mx = fmaxf(mx, fmaxf(s0, s1)); Sp[30 + (j >> 2)][j & 3] = cvtpk(s0, s1); }
    STAMP(4);
    {
      auto rr = __builtin_amdgcn_permlane32_swap(__float_as_uint(mx), __float_as_uint(mx), false, false);
      mx = fmaxf(__uint_as_float(rr[0]), __uint_as_float(rr[1]));
    }
    const float cs = 0.09016844005556021f;
    const f32x2 cs2 = {cs, cs}, nm2 = {-mx * cs, -mx * cs};
    f32x2 l2 = {0.f, 0.f};
#pragma unroll
    for (int j = 0; j < 128; j++) {
      const unsigned u = Sp[j >> 2][j & 3];
      f32x2 v = {__uint_as_float(u << 16), __uint_as_float(u & 0xffff0000u)};
      v = __builtin_elementwise_fma(v, cs2, nm2);
      f32x2 e = {__builtin_amdgcn_exp2f(v[0]), __builtin_amdgcn_exp2f(v[1])};
      l2 += e;
      Sp[j >> 2][j & 3] = cvtpk(e[0], e[1]);
      if ((j & 7) == 7) { asm volatile("" : "+v"(l2), "+v"(Sp[j >> 2]), "+v"(Sp[(j >> 2) - 1])); SB(); }
    }
    float l = l2[0] + l2[1];
    {
      auto rr = __builtin_amdgcn_permlane32_swap(__float_as_uint(l), __float_as_uint(l), false, false);
      l = __uint_as_float(rr[0]) + __uint_as_float(rr[1]);
    }
    const float inv = 1.0f / l;
    bf16x8 P[32];
#pragma unroll
    for (int j = 0; j < 32; j++) P[j] = __builtin_bit_cast(bf16x8, Sp[j]);
    STAMP(5);
    unsigned aV[3][2], aO[3][4];
#pragma unroll
    for (int gq = 0; gq < 3; gq++) {
#pragma unroll
      for (int e = 0; e < 2; e++) {
        aV[gq][e] = (((4u * hh + q4) * 128u + 64u * (q4 >> 1) + 16u * (2u * dhalf + (p >> 1)) + 8u * (p & 1)) ^ (64u * e)) + gq * 65536u;
        asm volatile("" : "+v"(aV[gq][e]));
      }
#pragma unroll
      for (int e = 0; e < 4; e++) {
        aO[gq][e] = (4u * hh + q4) * 256u + 64u * (e ^ q4) + 16u * (2u * dhalf + (p >> 1)) + 8u * (p & 1) + gq * 65536u;
        asm volatile("" : "+v"(aO[gq][e]));
      }
    }
    unsigned aB[3];
#pragma unroll
    for (int gq = 0; gq < 3; gq++) { aB[gq] = lane16 + w * 4096u + gq * 65536u; asm volatile("" : "+v"(aB[gq])); }
    auto ldF = [&](int i, int k) -> bf16x8 {
      const int s = (oPos(i) + 20) % 9;
      if (kOrd[i] < 16) {
        const unsigned o = (s & 3) * SLOT + (k >> 2) * 4096u + ((k >> 1) & 1) * 2048u;
        return trpair(lds + aV[s >> 2][k & 1] + o, lds + aV[s >> 2][k & 1] + o + 1024u);
      } else {
        const unsigned o = (s & 3) * SLOT + (k >> 2) * 4096u;
        return trpair(lds + aO[s >> 2][k & 3] + o, lds + aO[s >> 2][k & 3] + o + 2048u);
      }
    };
    auto ldB = [&](int i, int u) -> bf16x8 { const int s = (oPos(i) - 1 + 20) % 9; return *(LAS bf16x8*)(lds + aB[s >> 2] + (s & 3) * SLOT + u * 1024u); };
    bf16x8 fB[2];
    waitU(17);
#pragma unroll
    for (int k = 0; k < 4; k++) fr[k] = ldF(0, k);
    sfor<32 * 16>([&](auto SG) {
      constexpr int sg = SG, t = sg >> 4, k = sg & 15, n = sg + 4, code = kOrd[t];
      SB();
      if constexpr (code < 16)      mfma_a<32 * (code >> 2) + 16 * (k & 1), ((code & 3) == 0 && k < 2)>(fr[sg & 7], P[2 * (4 * (code & 3) + (k >> 2)) + ((k >> 1) & 1)]);
      else if constexpr (code < 20) mfma_ab<192 + 16 * (k & 3), 128 + 16 * (code - 16) + 4 * (k >> 2), (t == oFirstOP() && k < 4)>(fr[sg & 7]);
      else                          mfma_a<192 + 16 * (k & 3), (t == oFirstOP() && k < 4)>(fr[sg & 7], fB[(k >> 2) & 1]);
      if constexpr (SPACEV > 0 && t <= oIndex(15)) asm volatile("s_nop %0" :: "i"(SPACEV - 1));
      SB();
      if constexpr (k == 12 && t < 31) waitU(16 + oPos(t < 31 ? t + 1 : 31) + 1);
      if constexpr (n < 512) fr[n & 7] = ldF(n >> 4, n & 15);
      if constexpr (code >= 20 && (k & 3) == 0 && k < 12) fB[((k >> 2) + 1) & 1] = ldB(t, (k >> 2) + 1);
      if constexpr (t < 31 && k == 12) { if constexpr (kOrd[t < 31 ? t + 1 : 31] >= 20) fB[0] = ldB(t + 1, 0); }
      if constexpr (k == 12) consumed(30 + oPos(t) + 1);
      constexpr int pc = tStep(t, 0), pf = tStep(t, 1);
      if constexpr (pc >= 0 && (k == 3 || k == 5 || k == 7 || k == 9)) {
        constexpr int u = (k - 3) >> 1, PA = 32 * pc + 8 * u;
        float r[8];
        asm volatile("v_accvgpr_read_b32 %0, a[%c8]\n\tv_accvgpr_read_b32 %1, a[%c8+1]\n\tv_accvgpr_read_b32 %2, a[%c8+2]\n\tv_accvgpr_read_b32 %3, a[%c8+3]\n\t"
                     "v_accvgpr_read_b32 %4, a[%c8+4]\n\tv_accvgpr_read_b32 %5, a[%c8+5]\n\tv_accvgpr_read_b32 %6, a[%c8+6]\n\tv_accvgpr_read_b32 %7, a[%c8+7]"
                     : "=v"(r[0]), "=v"(r[1]), "=v"(r[2]), "=v"(r[3]), "=v"(r[4]), "=v"(r[5]), "=v"(r[6]), "=v"(r[7]) : "i"(PA));
        u32x4 wv = {cvtpk(r[0] * inv, r[1] * inv), cvtpk(r[2] * inv, r[3] * inv), cvtpk(r[4] * inv, r[5] * inv), cvtpk(r[6] * inv, r[7] * inv)};
        __builtin_amdgcn_raw_buffer_store_b128(wv, srdOw, lane16, (pc * 3 + w) * 4096 + u * 1024, 16);
        acc_wr4<128 + 16 * (pc >= 0 ? pc : 0) + 4 * u>(wv);
      }
      if constexpr (pf >= 0 && k == kSF[pf >= 0 ? pf : 0]) {
        flagstN<vmBetween(16 * kTS[pf >= 0 ? pf : 0] + 9, 16 * t + k)>(foMine + pf * 192, tag);
        STAMP(6 + pf);
      }
      if constexpr (code == 15 && k == 15) STAMP(13);
      if constexpr (code == 19 && k == 15) STAMP(15);
    });
    SB();

    STAMP(10);
    MFMA_DRAIN();
    {
      const int grp = (br * 8 + b) * 4 + jn;
      const __amdgpu_buffer_rsrc_t srdT = __builtin_amdgcn_make_buffer_rsrc((void*)(ws + T_OFF + (size_t)grp * 196608), (short)0, 196608, 0x00020000);
      const int nt = jn == 3 ? 2 : 4;
      f32x16 own;
      sfor<4>([&](auto NU) {
        constexpr int nu = NU;
        if (nu < nt) {
          const f32x16 tl = acc_rd16<192 + 16 * nu>();
          if (nu != jk) {
#pragma unroll
            for (int g4 = 0; g4 < 4; g4++) {
              u32x4 wv = {__float_as_uint(tl[4 * g4]), __float_as_uint(tl[4 * g4 + 1]), __float_as_uint(tl[4 * g4 + 2]), __float_as_uint(tl[4 * g4 + 3])};
              __builtin_amdgcn_raw_buffer_store_b128(wv, srdT, lane16, ((nu * 4 + jk) * 3 + w) * 4096 + g4 * 1024, 16);
            }
          } else own = tl;
        }
      });
      char* ftOut = ws + WS_FT + ((size_t)((grp * 4 + (lane & 3)) * 4 + jk) * 3 + w) * 64;
      const unsigned long long mask = (nt == 4 ? 0xfull : 0x3ull) & ~(1ull << jk);
      asm volatile("s_waitcnt vmcnt(0)\n\ts_mov_b64 exec, %2\n\tglobal_store_dwordx2 %0, %1, off sc1\n\ts_mov_b64 exec, -1" :: "v"(ftOut), "v"(tag), "s"(mask) : "memory");
      if (jk < nt) {
        const char* ftIn = ws + WS_FT + ((size_t)((grp * 4 + jk) * 4 + ((lane & 3) == jk ? ((jk + 1) & 3) : (lane & 3))) * 3 + w) * 64;
        poll(ftIn);
        STAMP(11);
#pragma unroll
        for (int s = 1; s < 4; s++) {
          const int src = (jk + s) & 3;
#pragma unroll
          for (int g4 = 0; g4 < 4; g4++) {
            const u32x4 wv = __builtin_amdgcn_raw_buffer_load_b128(srdT, lane16, ((jk * 4 + src) * 3 + w) * 4096 + g4 * 1024, 16);
#pragma unroll
            for (int e = 0; e < 4; e++) own[4 * g4 + e] += __uint_as_float(wv[e]);
          }
        }
        u16* orow = out + (size_t)(b * 96 + 32 * w + l31) * 896 + br * 448 + 128 * jn + 32 * jk + 4 * hh;
#pragma unroll
        for (int g4 = 0; g4 < 4; g4++) {
          const u32x2 o2 = {cvtpk(own[4 * g4], own[4 * g4 + 1]), cvtpk(own[4 * g4 + 2], own[4 * g4 + 3])};
          asm volatile("global_store_dwordx2 %0, %1, off" POLOUT :: "v"(orow + 8 * g4), "v"(o2) : "memory");
        }
      }
      if (h == 0 && w == 0 && lane == 0) *(unsigned long long*)(ws + WS_E + (br * 8 + b) * 128) = tag;
    }
  }
}

extern "C" void kernel_launch(void* const* d_in, const int* in_sizes, int n_in, void* d_out, int out_size, void* d_ws, size_t ws_size, hipStream_t stream) {
  (void)hipFuncSetAttribute((const void*)fused_attn, hipFuncAttributeMaxDynamicSharedMemorySize, LDS_BYTES);
  fused_attn<<<256, 256, LDS_BYTES, stream>>>((const u16*)d_in[0], (const u16*)d_in[1], (const u16*)d_in[2], (const u16*)d_in[3],
                                             (const u16*)d_in[5], (const u16*)d_in[6], (const u16*)d_in[7], (const u16*)d_in[8],
                                             (const u16*)d_in[4], (const u16*)d_in[9], (u16*)d_out, (char*)d_ws);
}
```
